```python
import torch

def _softmax(x, axis=-1):
    return torch.softmax(x, dim=axis)
def _gelu(x):
    return torch.nn.functional.gelu(x, approximate="tanh")
def _silu(x):
    return torch.nn.functional.silu(x)
def _sigmoid(x):
    return torch.sigmoid(x)
def _layernorm(x, axis=-1, eps=1e-5):
    m = x.mean(dim=axis, keepdim=True)
    v = x.var(dim=axis, keepdim=True, unbiased=False)
    return (x - m) / torch.sqrt(v + eps)
def _scatter_add(tgt, idx, src, axis=0):
    return tgt.clone().index_add_(axis, idx, src)
def _segment_sum(src, seg, n):
    out = torch.zeros((n,) + src.shape[1:], dtype=src.dtype)
    return out.index_add_(0, seg, src)
def _pad(x, pw):
    flat = tuple(v for pair in reversed(pw) for v in pair)
    return torch.nn.functional.pad(x, flat)

kernel_name = 'recurrent_scan__spectral_gfnet__mixer_channel_mlp__prompt'

def setup_inputs():

    _w = (lambda a: (lambda t: t.to(torch.bfloat16) if t.is_floating_point() else t)(torch.as_tensor(a)))
    def _A_setup():
        B, S, D = 1, 2048, 576
        Df = D // 2 + 1
        return {
            "x": _w((lambda *s: torch.randn(*s, dtype=torch.bfloat16) * 0.05)(B, S, D)),
            "w_r": _w((lambda *s: torch.randn(*s, dtype=torch.bfloat16) * 0.8)(S, Df) * 0.02),
            "w_i": _w((lambda *s: torch.randn(*s, dtype=torch.bfloat16) * 0.8)(S, Df) * 0.02),
        }
    _A_in = _A_setup()

    def _B_setup():
        B, S, D, F = 1, 2048, 576, 4608
        return {
            "x": _w((lambda *s: torch.randn(*s, dtype=torch.bfloat16) * 0.05)(B, S, D)),
            "w1": _w((lambda *s: torch.randn(*s, dtype=torch.bfloat16) * 0.8)(D, F) * (D ** -0.5)),
            "b1": _w((lambda *s: torch.randn(*s, dtype=torch.bfloat16) * 0.8)(F)),
            "w2": _w((lambda *s: torch.randn(*s, dtype=torch.bfloat16) * 0.8)(F, D) * (F ** -0.5)),
            "b2": _w((lambda *s: torch.randn(*s, dtype=torch.bfloat16) * 0.2)(D)),
        }
    _B_in = _B_setup()

    return {
        "A_x": _A_in["x"],
        "A_w_r": _A_in["w_r"],
        "A_w_i": _A_in["w_i"],
        "B_x": _B_in["x"],
        "B_w1": _B_in["w1"],
        "B_b1": _B_in["b1"],
        "B_w2": _B_in["w2"],
        "B_b2": _B_in["b2"],
    }

def reference(A_x, A_w_r, A_w_i, B_x, B_w1, B_b1, B_w2, B_b2):
    def _A_ref(x, w_r, w_i):
        B, S, D = x.shape
        X = (lambda x, axes=(-2,-1): torch.fft.rfft2(x.float(), dim=axes))(x, axes=(-2, -1))
        W = w_r + 1j * w_i
        X = X * W
        y = (lambda x, s, axes=(-2,-1): torch.fft.irfft2(x, s=s, dim=axes).to(torch.bfloat16))(X, (S, D), axes=(-2, -1))
        return y

    def _B_ref(x, w1, b1, w2, b2):
        y = _layernorm(x, axis=-1)
        h = _gelu(torch.einsum("bsd,df->bsf", y, w1) + b1)
        return torch.einsum("bsf,fd->bsd", h, w2) + b2

    x = A_x
    xn = _layernorm(x, axis=-1)
    s = _A_ref(xn, A_w_r, A_w_i)
    g = _silu(_B_ref(xn, B_w1, B_b1, B_w2, B_b2))
    return x + s * g
```

```cpp
#include <hip/hip_runtime.h>
#include <stdint.h>
#include <type_traits>
#include <utility>

typedef unsigned short u16;
typedef unsigned int u32;
typedef float f2 __attribute__((ext_vector_type(2)));
typedef float f4 __attribute__((ext_vector_type(4)));
typedef float f16v __attribute__((ext_vector_type(16)));
typedef short s4v __attribute__((ext_vector_type(4)));
typedef short s8v __attribute__((ext_vector_type(8)));
typedef u32 u4v __attribute__((ext_vector_type(4)));
typedef u32 u2v __attribute__((ext_vector_type(2)));
typedef __bf16 bf2v __attribute__((ext_vector_type(2)));
typedef __bf16 bf8v __attribute__((ext_vector_type(8)));
#define DI __device__ __forceinline__
#define LDS_AS __attribute__((address_space(3)))
#define GLB_AS __attribute__((address_space(1)))

#define K0_WARM 1
#define W2_WARM 0
#define P_AUX 16
constexpr int S_ = 2048, D_ = 576, DF_ = 289, F_ = 4608;

template <int N> using IC = std::integral_constant<int, N>;
template <int B, class Fn, int... I> DI void static_for_impl(Fn&& f, std::integer_sequence<int, I...>) { (f(IC<B + I>{}), ...); }
template <int B, int E, class Fn> DI void static_for(Fn&& f) { static_for_impl<B>(f, std::make_integer_sequence<int, E - B>{}); }

constexpr double kPi = 3.14159265358979323846264338327950288;
constexpr double c_sin_small(double x) {
  double x2 = x * x, t = x, s = x;
  for (int i = 1; i <= 9; ++i) { t *= -x2 / double((2 * i) * (2 * i + 1)); s += t; }
  return s;
}
constexpr double c_cos_small(double x) {
  double x2 = x * x, t = 1.0, s = 1.0;
  for (int i = 1; i <= 9; ++i) { t *= -x2 / double((2 * i - 1) * (2 * i)); s += t; }
  return s;
}
struct cf { float x, y; };
constexpr cf c_tw(int num, int den) {
  num %= den;
  int q = (4 * num + den / 2) / den;
  double r = 2.0 * kPi * (double(4 * num - q * den) / double(4 * den));
  double s = c_sin_small(r), c = c_cos_small(r);
  double cs = 0, sn = 0;
  switch (q & 3) {
    case 0: cs = c; sn = s; break;
    case 1: cs = -s; sn = c; break;
    case 2: cs = -c; sn = -s; break;
    default: cs = s; sn = -c; break;
  }
  return cf{float(cs), float(-sn)};
}
struct Tw2048 { cf v[512]; };
constexpr Tw2048 mk_2048(int q) {
  Tw2048 t{};
  for (int n = 0; n < 512; ++n) t.v[n] = c_tw(n * q, 2048);
  return t;
}
struct TwTail { cf a[4][64][2]; cf b[4][8][2]; };
constexpr TwTail mk_tail() {
  TwTail t{};
  for (int k = 1; k <= 8; ++k) {
    for (int l = 0; l < 64; ++l) t.a[(k - 1) / 2][l][(k - 1) & 1] = c_tw((2 * (l & 31) + (l >> 5)) * k, 576);
    for (int u = 0; u < 8; ++u) t.b[(k - 1) / 2][u][(k - 1) & 1] = c_tw(u * k, 64);
  }
  return t;
}
__device__ const TwTail g_twr __attribute__((aligned(16))) = mk_tail();
__device__ const Tw2048 g_tw2048_1 __attribute__((aligned(16))) = mk_2048(1);

DI float bf_lo(u32 v) { return __uint_as_float(v << 16); }
DI float bf_hi(u32 v) { return __uint_as_float(v & 0xffff0000u); }
DI u32 pack_bf2(float lo, float hi) {
  bf2v r; r.x = (__bf16)lo; r.y = (__bf16)hi;
  return __builtin_bit_cast(u32, r);
}
DI float rbf(float f) { return bf_lo(pack_bf2(f, 0.f)); }
DI f2 ld_tw(const cf* p) { return *(const f2*)p; }

template <int CTRL> DI float dpp_add(float v) {
  int t = __builtin_amdgcn_update_dpp(0, __float_as_int(v), CTRL, 0xf, 0xf, true);
  return v + __int_as_float(t);
}
DI float red32(float v) {
  v = dpp_add<0xB1>(v);
  v = dpp_add<0x4E>(v);
  v = dpp_add<0x141>(v);
  v = dpp_add<0x140>(v);
  auto r = __builtin_amdgcn_permlane16_swap(__float_as_uint(v), __float_as_uint(v), false, false);
  return __uint_as_float(r[0]) + __uint_as_float(r[1]);
}

DI f2 add_mi(f2 a, f2 b) {
  f2 r; asm("v_pk_add_f32 %0, %1, %2 op_sel:[0,1] op_sel_hi:[1,0] neg_hi:[0,1]" : "=v"(r) : "v"(a), "v"(b)); return r;
}
DI f2 add_pi(f2 a, f2 b) {
  f2 r; asm("v_pk_add_f32 %0, %1, %2 op_sel:[0,1] op_sel_hi:[1,0] neg_lo:[0,1]" : "=v"(r) : "v"(a), "v"(b)); return r;
}
DI f2 fma_mi(f2 b, f2 s, f2 a) {
  f2 r; asm("v_pk_fma_f32 %0, %1, %2, %3 op_sel:[1,0,0] op_sel_hi:[0,1,1] neg_hi:[1,0,0]" : "=v"(r) : "v"(b), "v"(s), "v"(a)); return r;
}
DI f2 fma_pi(f2 b, f2 s, f2 a) {
  f2 r; asm("v_pk_fma_f32 %0, %1, %2, %3 op_sel:[1,0,0] op_sel_hi:[0,1,1] neg_lo:[1,0,0]" : "=v"(r) : "v"(b), "v"(s), "v"(a)); return r;
}
DI f2 cmul(f2 a, f2 w) {
  f2 t, r;
  asm("v_pk_mul_f32 %0, %1, %2 op_sel:[1,1] op_sel_hi:[1,0] neg_lo:[0,1]" : "=v"(t) : "v"(a), "v"(w));
  asm("v_pk_fma_f32 %0, %1, %2, %3 op_sel:[0,0,0] op_sel_hi:[0,1,1]" : "=v"(r) : "v"(a), "v"(w), "v"(t));
  return r;
}
DI f2 cmulc(f2 a, f2 w) {
  f2 t, r;
  asm("v_pk_mul_f32 %0, %1, %2 op_sel:[1,1] op_sel_hi:[1,0]" : "=v"(t) : "v"(a), "v"(w));
  asm("v_pk_fma_f32 %0, %1, %2, %3 op_sel:[0,0,0] op_sel_hi:[0,1,1] neg_hi:[0,1,0]" : "=v"(r) : "v"(a), "v"(w), "v"(t));
  return r;
}
template <bool INV> DI f2 twm(f2 a, f2 w) { if constexpr (INV) return cmulc(a, w); else return cmul(a, w); }
template <bool INV> DI f2 addr_(f2 a, f2 b) { if constexpr (INV) return add_pi(a, b); else return add_mi(a, b); }
template <bool INV> DI f2 subr_(f2 a, f2 b) { if constexpr (INV) return add_mi(a, b); else return add_pi(a, b); }
template <bool INV> DI f2 fmar_(f2 b, f2 s, f2 a) { if constexpr (INV) return fma_pi(b, s, a); else return fma_mi(b, s, a); }
template <bool INV> DI f2 fmsr_(f2 b, f2 s, f2 a) { if constexpr (INV) return fma_mi(b, s, a); else return fma_pi(b, s, a); }

template <bool INV> DI void bfly4(f2& c0, f2& c1, f2& c2, f2& c3) {
  f2 d0 = c0 + c2, d1 = c0 - c2, d2 = c1 + c3, e3 = c1 - c3;
  c0 = d0 + d2; c2 = d0 - d2; c1 = addr_<INV>(d1, e3); c3 = subr_<INV>(d1, e3);
}
template <bool INV> DI void bfly8(f2 (&a)[8]) {
  const f2 r = {0.70710678118654752440f, 0.70710678118654752440f};
  f2 b0 = a[0] + a[4], b4 = a[0] - a[4];
  f2 b1 = a[1] + a[5], t5 = a[1] - a[5];
  f2 b2 = a[2] + a[6], t6 = a[2] - a[6];
  f2 b3 = a[3] + a[7], t7 = a[3] - a[7];
  bfly4<INV>(b0, b1, b2, b3);
  f2 f0 = addr_<INV>(b4, t6), f1 = subr_<INV>(b4, t6);
  f2 p = t5 + t7, m = t5 - t7;
  f2 g2 = addr_<INV>(m, p), g3 = addr_<INV>(p, m);
  a[0] = b0; a[2] = b1; a[4] = b2; a[6] = b3;
  a[1] = __builtin_elementwise_fma(g2, r, f0); a[5] = __builtin_elementwise_fma(-g2, r, f0);
  a[3] = fmar_<INV>(g3, r, f1); a[7] = fmsr_<INV>(g3, r, f1);
}
template <bool INV> DI void bfly3(f2& a, f2& b, f2& c) {
  const f2 s = {0.86602540378443864676f, 0.86602540378443864676f};
  const f2 hh = {-0.5f, -0.5f};
  f2 t = b + c, d = b - c, m = __builtin_elementwise_fma(t, hh, a);
  a = a + t; b = fmar_<INV>(d, s, m); c = fmsr_<INV>(d, s, m);
}
template <bool INV> DI void bfly9(f2 (&a)[9]) {
  bfly3<INV>(a[0], a[3], a[6]);
  bfly3<INV>(a[1], a[4], a[7]);
  bfly3<INV>(a[2], a[5], a[8]);
  const f2 w1 = {0.76604444311897803520f, -0.64278760968653932632f};
  const f2 w2 = {0.17364817766693034885f, -0.98480775301220805937f};
  const f2 w4 = {-0.93969262078590838405f, -0.34202014332566873304f};
  a[4] = twm<INV>(a[4], w1); a[7] = twm<INV>(a[7], w2);
  a[5] = twm<INV>(a[5], w2); a[8] = twm<INV>(a[8], w4);
  bfly3<INV>(a[0], a[1], a[2]);
  bfly3<INV>(a[3], a[4], a[5]);
  bfly3<INV>(a[6], a[7], a[8]);
  f2 t1 = a[1], t2 = a[2], t5 = a[5];
  a[1] = a[3]; a[2] = a[6]; a[3] = t1; a[5] = a[7]; a[6] = t2; a[7] = t5;
}

DI void ln18(const float (&v)[18], float (&o)[18], bool last, u32 (&pk)[9]) {
  float s = 0.f;
#pragma unroll
  for (int i = 0; i < 18; ++i) s += v[i];
  const float m = red32(s) * (1.0f / D_);
  float q = 0.f;
#pragma unroll
  for (int i = 0; i < 18; ++i) { float d = v[i] - m; q += d * d; }
  const float var = red32(q) * (1.0f / D_);
  const float mb = rbf(m);
  const float sd = rbf(__builtin_sqrtf(rbf(rbf(var) + 1e-5f)));
  const float inv = 1.0f / sd;
#pragma unroll
  for (int j = 0; j < 9; ++j) {
    u32 d = pack_bf2(v[2 * j] - mb, v[2 * j + 1] - mb);
    u32 r = pack_bf2(bf_lo(d) * inv, bf_hi(d) * inv);
    pk[j] = r;
    if (!last) { o[2 * j] = bf_lo(r); o[2 * j + 1] = bf_hi(r); }
  }
}

#define STAMP(i)
#define RSTAMP(i)
#define STAMP_DECL
#define STAMP_OUT(base, kidx, nw, n)

#define WAVE_SYNC() do { __builtin_amdgcn_fence(__ATOMIC_RELEASE, "wavefront"); __builtin_amdgcn_wave_barrier(); __builtin_amdgcn_fence(__ATOMIC_ACQUIRE, "wavefront"); } while (0)
template <class T> DI void PIN(T& x) { asm volatile("" : "+v"(x)); }
template <class T> DI void SPIN(T& x) { asm volatile("" : "+s"(x)); }
template <class T> DI void KEEP(const T& x) { asm volatile("" :: "v"(x)); }
#define RAW_BAR() do { asm volatile("s_waitcnt lgkmcnt(0)" ::: "memory"); __builtin_amdgcn_s_barrier(); asm volatile("" ::: "memory"); } while (0)

DI u4v mk_srd(const void* p, u32 bytes) {
  const uint64_t a = (uint64_t)p;
  return u4v{(u32)a, (u32)(a >> 32) & 0xffffu, bytes, 0x00020000u};
}
DI void l2_touch(u32 lds_sink, u32 voff, u4v srd) {
  asm volatile("s_mov_b32 m0, %0\n\ts_nop 0\n\tbuffer_load_dword %1, %2, 0 offen lds" :: "s"(lds_sink), "v"(voff), "s"(srd) : "memory");
}
DI void lds_sync(LDS_AS u32* c, u32 n, int lane) {
  asm volatile("s_waitcnt lgkmcnt(0)" ::: "memory");
  if (lane == 0) __hip_atomic_fetch_add(c, 1u, __ATOMIC_RELAXED, __HIP_MEMORY_SCOPE_WORKGROUP);
  while (__builtin_amdgcn_readfirstlane(__hip_atomic_load(c, __ATOMIC_RELAXED, __HIP_MEMORY_SCOPE_WORKGROUP)) < n) __builtin_amdgcn_s_sleep(1);
  asm volatile("" ::: "memory");
}

typedef __attribute__((address_space(1))) u32 gu32;
#define RLX_AGENT __ATOMIC_RELAXED, __HIP_MEMORY_SCOPE_AGENT
DI void wait_count(gu32* c, u32 want) {
#pragma nounroll
  for (u32 spins = 0; spins < (1u << 20); ++spins) {
    if ((u32)__builtin_amdgcn_readfirstlane(__hip_atomic_load(c, RLX_AGENT)) >= want) break;
    __builtin_amdgcn_s_sleep(2);
  }
  __builtin_amdgcn_fence(__ATOMIC_ACQUIRE, "wavefront");
  asm volatile("" ::: "memory");
}
constexpr size_t O_Y = 0, O_Z1 = O_Y + (size_t)S_ * D_ * 2, O_WT = O_Z1 + (size_t)576 * 1024 * 8, O_Z2 = O_WT + (size_t)DF_ * 2048 * 4,
                 O_P = O_Z2 + (size_t)576 * 1024 * 8, O_SYNC = O_P + (size_t)16 * S_ * D_ * 2;
constexpr int SYNC_STRIDE = 64;
constexpr int N_FFT_WG = 256;

__global__ __launch_bounds__(320) void k0_prep(const u16* __restrict__ x, const u16* __restrict__ wr, const u16* __restrict__ wi,
                                               const u16* __restrict__ w1, char* __restrict__ ws) {
  __shared__ __attribute__((aligned(16))) f2 buf[9 * 256 + 32];
  u4v* __restrict__ Yf = (u4v*)(ws + O_Y); f2* __restrict__ Z1T = (f2*)(ws + O_Z1); u32* __restrict__ WT = (u32*)(ws + O_WT);
  u32* __restrict__ sync = (u32*)(ws + O_SYNC);
  STAMP_DECL; RSTAMP(30); STAMP(0);
  const int b = blockIdx.x & 255, rb = (b & 7) * 32 + (b >> 3);
  const bool typeA = blockIdx.x < 256;
  const int tid = threadIdx.x, lane = tid & 63;
  const int w = __builtin_amdgcn_readfirstlane(tid >> 6);
  const int l31 = lane & 31, hi = lane >> 5;

  u32 xr[9];
  const int row = rb * 8 + (w & 3) * 2 + hi;
  if (w < 4) {
    const u32* xp = (const u32*)(x + (size_t)row * D_) + l31;
#pragma unroll
    for (int j = 0; j < 9; ++j) xr[j] = xp[32 * j];
  }
  if (typeA) {
    const int k9 = tid >> 5, t = (tid >> 2) & 7, fw = tid & 3;
    f2 tw64[8];
#pragma unroll
    for (int k = 0; k < 4; ++k) { const f4 v = *(const f4*)&g_twr.b[k][t][0]; tw64[2 * k] = v.xy; tw64[2 * k + 1] = v.zw; }
    if (w < 4) {
      f2 tw[8];
#pragma unroll
      for (int k = 0; k < 4; ++k) { const f4 v = *(const f4*)&g_twr.a[k][lane][0]; tw[2 * k] = v.xy; tw[2 * k + 1] = v.zw; }
      asm volatile("" ::: "memory");
      float v[18], o[18]; u32 pk[9];
#pragma unroll
      for (int j = 0; j < 9; ++j) { v[2 * j] = bf_lo(xr[j]); v[2 * j + 1] = bf_hi(xr[j]); }
      STAMP(1);
      ln18(v, o, false, pk);
      f2 a[9];
#pragma unroll
      for (int j = 0; j < 9; ++j) {
        auto r = __builtin_amdgcn_permlane32_swap(__float_as_uint(o[2 * j]), __float_as_uint(o[2 * j + 1]), false, false);
        a[j] = f2{__uint_as_float(r[0]), __uint_as_float(r[1])};
      }
      STAMP(2);
      bfly9<false>(a);
      const int n0 = 2 * l31 + hi;
      const int i = n0 >> 3, e = (n0 & 7) * 4 + w;
      const int pos = i * 32 + (e ^ ((e >> 4) << 2) ^ (i & 3));
      buf[pos] = a[0];
#pragma unroll
      for (int k = 1; k < 9; ++k) buf[k * 256 + pos] = cmul(a[k], tw[k - 1]);
    } else {
      STAMP(1); STAMP(2);
#pragma unroll
      for (int k = 0; k < 7; ++k) PIN(tw64[k]);
      const u32 sink = (u32)(uintptr_t)(LDS_AS f2*)(buf + 9 * 256);
      {
        uint64_t pc; u32 len; int fwd;
        asm volatile("s_getpc_b64 %0\n.Lk0_pc:\n\ts_mov_b32 %1, .Lk1_end-.Lk1_begin\n\ts_mov_b32 %2, .Lk1_begin-.Lk0_pc" : "=s"(pc), "=s"(len), "=s"(fwd));
        pc += fwd;
        if ((u32)(b >> 3) * 8192 < len) l2_touch(sink, (u32)((b >> 3) * 64 + lane) * 128, u4v{(u32)pc, (u32)(pc >> 32) & 0xffffu, len, 0x00020000u});
      }
      const u4v srd = mk_srd(w1, (u32)D_ * F_ * 2);
      const u32 base = (u32)((b >> 3) * 18) * (F_ * 2) + (u32)((b & 7) >> 1) * 2304;
#pragma unroll
      for (int i = 0; i < 3; ++i) {
        u32 sct = i * 64 + lane; sct = sct < 180 ? sct : 179;
        const u32 r = sct / 10, c = sct - r * 10, c5 = c >= 5 ? c - 5 : c;
        l2_touch(sink, base + r * (F_ * 2) + (c >= 5 ? 1152 : 0) + (c5 >> 1) * 512 + (c5 & 1) * 128, srd);
      }
    }
    STAMP(3);
    RAW_BAR();
    STAMP(4);
    if (tid < 288) {
      f2* rg = buf + k9 * 256;
      f2 a[8];
      const int e = t * 4 + fw, e2 = e ^ ((e >> 4) << 2);
#pragma unroll
      for (int i = 0; i < 8; ++i) a[i] = rg[i * 32 + (e2 ^ (i & 3))];
      bfly8<false>(a);
      rg[(t * 4) + fw] = a[0];
#pragma unroll
      for (int k = 1; k < 8; ++k) rg[k * 32 + ((t ^ k) * 4 + fw)] = cmul(a[k], tw64[k - 1]);
      WAVE_SYNC();
#pragma unroll
      for (int i = 0; i < 8; ++i) a[i] = rg[t * 32 + ((i ^ t) * 4 + fw)];
      bfly8<false>(a);
      f2* zp = Z1T + (size_t)(k9 + 9 * t) * 1024 + rb * 4 + fw;
#pragma unroll
      for (int kb = 0; kb < 8; ++kb) zp[(size_t)kb * 72 * 1024] = a[kb];
    }
  } else {
    char* yb = (char*)buf;
    LDS_AS u32* cnt = (LDS_AS u32*)(buf + 9 * 256);
    if (tid == 0) *cnt = 0;
    RAW_BAR();
    if (w < 4) {
      float v[18], o[18], dummy[18]; u32 pk[9];
#pragma unroll
      for (int j = 0; j < 9; ++j) { v[2 * j] = bf_lo(xr[j]); v[2 * j + 1] = bf_hi(xr[j]); }
      STAMP(1);
      ln18(v, o, false, pk);
      ln18(o, dummy, true, pk);
#pragma unroll
      for (int j = 0; j < 9; ++j) *(u32*)(yb + ((l31 >> 2) + 8 * j) * 144 + (w * 2 + hi) * 16 + (l31 & 3) * 4) = pk[j];
      STAMP(2);
    } else {
      if (b == 0 && lane < 33) sync[lane * SYNC_STRIDE] = 0;
      if (lane < 37) {
        typedef u4v u4u __attribute__((aligned(2)));
        const int k2 = lane < 36 ? lane * 8 : DF_ - 8;
        const int base = (rb >> 6) + 4 * ((rb >> 3) & 7) + 32 * (rb & 7);
        u4v wa[8], wb[8];
#pragma unroll
        for (int c = 0; c < 8; ++c) {
          const size_t off = (size_t)(base + 256 * c) * DF_ + k2;
          wa[c] = *(const u4u*)(wr + off); wb[c] = *(const u4u*)(wi + off);
        }
        STAMP(1);
#pragma unroll
        for (int e = 0; e < 8; ++e) {
          u32 o[8];
#pragma unroll
          for (int c = 0; c < 8; ++c) o[c] = __builtin_amdgcn_perm(wb[c][e >> 1], wa[c][e >> 1], (e & 1) ? 0x07060302u : 0x05040100u);
          u4v* wp = (u4v*)(WT + ((size_t)(k2 + e) * 256 + rb) * 8);
          wp[0] = u4v{o[0], o[1], o[2], o[3]};
          wp[1] = u4v{o[4], o[5], o[6], o[7]};
        }
      }
      STAMP(2);
    }
    STAMP(3);
    if (w < 4) lds_sync(cnt, 4u, lane);
    STAMP(4);
    u4v* yp = Yf + (size_t)(rb >> 2) * 72 * 32 + (rb & 3) * 8;
    if (tid < 192) {
#pragma unroll
      for (int r = 0; r < 3; ++r) {
        const int i = tid + 192 * r;
        yp[(i >> 3) * 32 + (i & 7)] = *(const u4v*)(yb + (i >> 3) * 144 + (i & 7) * 16);
      }
    }
  }
}

constexpr int SLOT = 36864;
constexpr int L_H = 3 * SLOT;
constexpr int L_B1 = L_H + 49152;
constexpr int L_DUMMY = L_B1 + 1280;
constexpr int L_CNT = L_DUMMY + 256;
constexpr int L_TOTAL = L_CNT + 32;
constexpr int PF = 4;
#define DLY 0, 0, 0, 0, 0, 0, 0, 0

constexpr int NS(int iv, int pos) { return iv * 18 + pos; }
enum { VM_NONE = 0, VM_DMA, VM_ST, VM_Y, VM_TL, VM_PF };
constexpr int N_POLL = 13 * 18 + 2, N_CHECK = 15 * 18 + 2;
constexpr int NTL = 25;
struct VmOp { int kind, a, b; };
constexpr VmOp vm_at(int n) {
  const int iv = n / 18, pos = n % 18;
  if (iv == 0) {
    if (pos < 5) return {VM_DMA, 1, pos};
    if (pos < 14) return {VM_Y, 2 * (pos - 5) + 1, 0};
    return {VM_DMA, 2, pos - 14};
  }
  if (iv == 1) {
    if (pos == 0) return {VM_DMA, 2, 4};
    if (pos < 10) return {VM_Y, 19 + 2 * (pos - 1), 0};
    if (pos < 15) return {VM_DMA, 3, pos - 10};
    return {VM_NONE, 0, 0};
  }
  if (iv + 2 < 18 && pos % 3 == 1 && pos < 15) return {VM_DMA, iv + 2, pos / 3};
  if (iv == 12 && pos >= 6) { const int k = pos == 6 ? 0 : pos == 8 ? 1 : pos == 11 ? 2 : pos == 14 ? 3 : pos == 15 ? 4 : pos == 17 ? 5 : -1; if (k >= 0) return {VM_ST, k, 0}; }
  if (iv >= 13 && iv <= 17) { if (pos == 6) return {VM_ST, 2 * (iv - 10), 0}; if (pos == 11) return {VM_ST, 2 * (iv - 10) + 1, 0}; }
  if (iv == 18) { if (pos == 6) return {VM_ST, 16, 0}; if (pos == 7) return {VM_ST, 17, 0}; }
  if (n == N_POLL) return {VM_TL, -1, 0};
  if (iv >= 3 && iv <= 5 && pos == 15) return {VM_PF, iv - 3, 0};
  if (iv == 14 || iv == 15) { const int k = pos == 2 ? 0 : pos == 5 ? 1 : pos == 8 ? 2 : pos == 12 ? 3 : pos == 15 ? 4 : pos == 17 ? 5 : -1; if (k >= 0) return {VM_TL, 8 + (iv - 14) * 6 + k, 0}; }
  if (iv == 16) { if (pos < 5) return {VM_TL, 20 + pos, 0}; if (pos >= 8 && (pos & 1) == 0) return {VM_TL, (pos - 8) / 2, 0}; }
  if (iv == 17 && pos < 6 && (pos & 1) == 0) return {VM_TL, 5 + pos / 2, 0};
  return {VM_NONE, 0, 0};
}
constexpr int vm_step_of(int kind, int a, int b) {
  for (int n = 0; n < 340; ++n) { const VmOp o = vm_at(n); if (o.kind == kind && o.a == a && (kind != VM_DMA || o.b == b)) return n; }
  return -1;
}
constexpr int vm_between(int n0, int n1) { int c = 0; for (int n = n0; n < n1; ++n) c += vm_at(n).kind != VM_NONE; return c; }
constexpr int vm_wait(int iv) { return vm_between(vm_step_of(VM_DMA, iv, 4) + 1, NS(iv, 0)); }
constexpr int vm_wait_y(int i, int n) {
  return (i & 1) ? vm_between(vm_step_of(VM_Y, i, 0) + 1, n + 1) : (17 - i / 2) + vm_between(0, n + 1);
}
constexpr int g_start(int job) { return job == 0 ? NS(2, 5) : NS(2 * job + 1, 6); }
constexpr int g_elem(int job, int e) { return g_start(job) + (job == 4 ? (5 * e) / 4 : 2 * e); }
constexpr int g_write(int job, int s) { return g_elem(job, 8 * s + 7) + 9; }
constexpr int h_wr(int job, int s) {
  const int n = g_write(job, s), iv = n / 18, pos = n % 18;
  return pos >= 15 ? n : NS(iv, 15 + ((job == 4 || job == 0) ? s : 0));
}
static_assert(g_write(4, 1) < NS(11, 0) && g_write(3, 1) < NS(10, 0) && g_write(2, 1) < NS(8, 0), "gelu deadline");

__global__ __launch_bounds__(512, 2) void k1_main(const u16* w1, const u16* b1, const u16* w2, const u16* b2, const u16* x, char* ws, u16* out) {
  extern __shared__ __attribute__((aligned(16))) char smem[];
  LDS_AS char* lds = (LDS_AS char*)smem;
  asm volatile(".Lk1_begin:");
  SPIN(w1); SPIN(b1); SPIN(w2); SPIN(b2); SPIN(x); SPIN(ws); SPIN(out);
  STAMP_DECL; RSTAMP(30); STAMP(0);
  const u4v* Yf = (const u4v*)(ws + O_Y); const f2* Z1T = (const f2*)(ws + O_Z1); const u4v* WT = (const u4v*)(ws + O_WT);
  f2* Z2T = (f2*)(ws + O_Z2); u4v* P = (u4v*)(ws + O_P); u32* sync = (u32*)(ws + O_SYNC);
  const int b = blockIdx.x;
  const int tid = threadIdx.x, lane = tid & 63;
  const int w = __builtin_amdgcn_readfirstlane(tid >> 6);
  const int xg = b & 7, idx = b >> 3, grp = idx >> 2;
  const int fs = 4 * (xg >> 1) + (idx & 3), mt = 8 * (xg & 1) + grp;
  const int mi = w & 3, hf = w >> 2;
  const int h = lane >> 5, blk = (lane >> 4) & 1, q = (lane >> 2) & 3, p4 = lane & 3;
  const int fpan = 576 * (fs >> 1) + 256 * (fs & 1);
  const int fsgl = 576 * (fs >> 1) + 512 + 32 * (fs & 1);

#define G_ONE 2
#define G_TWO 5
  constexpr int N1 = 256 - 32 * G_ONE;
  const int task = hf == 0 ? (grp >= G_ONE ? b - 32 * G_ONE : DF_) : N1 + (255 - b);
  const bool act = task < DF_;
  const int t = tid & 255;
  f2 zp_[4], zq_[4], twb1, twc1;
  u4v wv0, wv1;
  f4 twa4[3];
  auto gl8 = [](f2& dst, const void* sb, u32 vo) { asm volatile("global_load_dwordx2 %0, %1, %2" : "=v"(dst) : "v"(vo), "s"(sb) : "memory"); };
  auto gl16 = [](auto& dst, const void* sb, u32 vo) { asm volatile("global_load_dwordx4 %0, %1, %2" : "=v"(dst) : "v"(vo), "s"(sb) : "memory"); };
  if (act) {
    const f2* c0 = Z1T + (size_t)task * 1024;
    const f2* c1 = Z1T + (size_t)(task == 0 ? 0 : 576 - task) * 1024;
#pragma unroll
    for (int j = 0; j < 4; ++j) { gl8(zp_[j], c0 + 256 * j, t * 8); gl8(zq_[j], c1 + 256 * j, t * 8); }
    gl16(twa4[0], &g_tw2048_1, t * 16);
    gl8(twb1, &g_tw2048_1, lane * 32);
    gl8(twc1, &g_tw2048_1, (lane & 7) * 256);
    gl16(wv0, WT + (size_t)task * 512, t * 32); gl16(wv1, WT + (size_t)task * 512 + 1, t * 32);
  }
  u32 bias_raw;
  {
    const int bt = tid >= 224 ? tid - 224 : 0;
    const u16* bp = tid < 64 ? b2 + (tid < 9 ? tid : 0) * 64 : b1 + (bt < 256 ? fpan + bt : fsgl + (bt - 256));
    asm volatile("global_load_ushort %0, %1, off" : "=v"(bias_raw) : "v"(bp) : "memory");
  }
  if (tid < 8) *(LDS_AS u32*)(lds + L_CNT + tid * 4) = 0;

  const u32 lo_w1 = (u32)(lane >> 3) * (F_ * 2) + (u32)(((lane & 7) ^ (((lane >> 4) & 1) << 2)) * 16);
  const u32 lo_w2 = (u32)(lane >> 3) * (D_ * 2) + (u32)(((lane & 7) ^ (((lane >> 4) & 1) << 2)) * 16);
  const u32 lo_ws = (u32)(lane >> 2) * (F_ * 2) + (u32)((lane & 3) * 16);
  const u32 lds0 = (u32)(uintptr_t)lds;
  const u4v srd1 = mk_srd(w1, (u32)D_ * F_ * 2), srd2 = mk_srd(w2, (u32)D_ * F_ * 2);
  u32 sb_ws = (u32)(w * 64 * F_ + fsgl) * 2, sb_wsh = (u32)((512 + w * 8) * F_ + fsgl) * 2;
  u32 sb_w1 = (u32)(w * 32 * F_ + fpan) * 2, sb_w1h = (u32)((256 + w * 4) * F_ + fpan) * 2;
  const int w3 = w / 3, wm3 = w - 3 * w3;
  u32 sb_w2 = (u32)((w == 0 ? fsgl : fpan + 32 * (w - 1)) * D_) * 2, sb_w2h = (u32)((fpan + 224 + 4 * w) * D_) * 2;
  u32 sb_j = (u32)((fpan + 32 * wm3 - 32) * D_ + 64 * w3) * 2;
  u32 sb_j0 = (u32)((wm3 == 0 ? fsgl : fpan + 32 * wm3 - 32) * D_ + 64 * w3) * 2;
  u32 sl_w = lds0 + w * 4096, sl_h = lds0 + 32768 + w * 512;
  SPIN(sb_w1); SPIN(sb_w1h); SPIN(sb_w2); SPIN(sb_w2h); SPIN(sb_j); SPIN(sb_j0); SPIN(sl_w); SPIN(sl_h);
  auto dma = [&](auto ST, auto NN) {
    constexpr int st = decltype(ST)::value, n = decltype(NN)::value;
    constexpr int slot = (st % 3) * SLOT;
    constexpr int nn = n < 4 ? n : 0;
    u32 sbase, voff;
    constexpr int p = (st - 1) >> 1, half = (st - 1) & 1;
    constexpr int imm = st == 0 ? nn * 16 * F_ * 2
                      : st <= 8 ? (half * 288 + nn * 8) * F_ * 2 + 128 * p
                      : st <= 11 ? (n < 4 ? ((st - 9) * 96 + nn * 8) * D_ * 2 : (32 * (3 * (st - 9) + 1) - 224) * D_ * 2 + 256)
                      : nn * 8 * D_ * 2 + 128 * (st - 9);
    if constexpr (st == 0) { sbase = n < 4 ? sb_ws : sb_wsh; voff = lo_ws; }
    else if constexpr (st <= 8) { sbase = n < 4 ? sb_w1 : sb_w1h; voff = lo_w1; }
    else if constexpr (st <= 11) { sbase = n < 4 ? (st == 9 ? sb_j0 : sb_j) : sb_w2h; voff = lo_w2; }
    else { sbase = n < 4 ? sb_w2 : sb_w2h; voff = lo_w2; }
    const u32 lbase = n < 4 ? sl_w : sl_h;
    const u4v srd = (st <= 8) ? srd1 : srd2;
    u32 tmp;
    if constexpr (n < 4)
      asm volatile("s_add_u32 m0, %1, %2\n\ts_add_u32 %0, %3, %4\n\tbuffer_load_dwordx4 %5, %6, %0 offen lds"
                   : "=&s"(tmp) : "s"(lbase), "n"(slot + nn * 1024), "s"(sbase), "n"(imm), "v"(voff), "s"(srd) : "memory", "scc");
    else
      asm volatile("s_add_u32 m0, %1, %2\n\ts_add_u32 %0, %3, %4\n\ts_mov_b32 exec_hi, 0\n\tbuffer_load_dwordx4 %5, %6, %0 offen lds\n\ts_mov_b32 exec_hi, -1"
                   : "=&s"(tmp) : "s"(lbase), "n"(slot), "s"(sbase), "n"(imm), "v"(voff), "s"(srd) : "memory", "scc");
  };
  u4v yf[36];
  const u4v ysrd = mk_srd(Yf + (size_t)(mt * 4 + mi) * 36 * 64, 36 * 1024);
  const u32 yve = lane * 16 + hf * 1024, yvo = lane * 16 + 1024 - hf * 1024;
  auto yload = [&](auto II) {
    constexpr int i = decltype(II)::value;
    const u4v srd = ysrd; const u32 vo = (i & 1) ? yvo : yve;
    u4v& dst = yf[i];
    asm volatile("buffer_load_dwordx4 %0, %1, %2, %3 offen offset:%4" : "=v"(dst) : "v"(vo), "s"(srd), "s"((i / 4) * 4096), "n"(((i >> 1) & 1) * 2048) : "memory");
  };
  auto bulk = [&](auto KK) { constexpr int k = decltype(KK)::value; if constexpr (k < 5) dma(IC<0>{}, IC<k>{}); else yload(IC<2 * (k - 5)>{}); };
#define BULK(a_, b_) static_for<a_, b_>(bulk)
  auto sync4 = [&](int which) { lds_sync((LDS_AS u32*)(lds + L_CNT + (hf * 2 + which) * 4), 4u, lane); };
  auto delay_until = [&](u32 n) { for (u32 i = 0; i < n; ++i) __builtin_amdgcn_s_sleep(8); };
  constexpr u32 dly[8] = {DLY};
  const u32 my_dly = grp == 0 ? dly[0] : grp == 1 ? dly[1] : grp == 2 ? dly[2] : grp == 3 ? dly[3] : grp == 4 ? dly[4] : grp == 5 ? dly[5] : grp == 6 ? dly[6] : dly[7];
  if (grp < G_ONE) BULK(0, 5);
  RAW_BAR();
  STAMP(1);
  if (act) {
    LDS_AS f2* fb = (LDS_AS f2*)(lds + L_H + hf * 16384);
    LDS_AS f2* wr_ = fb + mi * 512;
    f2 a[8], twb[8], twc[8];
    const int ka = lane >> 3, u = lane & 7;
    asm volatile("s_waitcnt vmcnt(1)" ::: "memory");
#pragma unroll
    for (int j = 0; j < 4; ++j) { PIN(zp_[j]); PIN(zq_[j]); }
    PIN(twa4[0]); PIN(twb1); PIN(twc1); PIN(wv0); PIN(wv1);
    STAMP(2);
    twa4[1].xy = cmul(twa4[0].xy, twa4[0].xy); twa4[1].zw = cmul(twa4[0].zw, twa4[0].zw);
    twa4[2].xy = cmul(twa4[1].xy, twa4[0].xy); twa4[2].zw = cmul(twa4[1].zw, twa4[0].zw);
    auto twa = [&](int k, int o) { return o ? twa4[k - 1].zw : twa4[k - 1].xy; };
#pragma unroll
    for (int j = 0; j < 4; ++j) {
      asm("v_pk_add_f32 %0, %1, %2 neg_hi:[0,1]" : "=v"(a[j]) : "v"(zp_[j]), "v"(zq_[j]));
      asm("v_pk_add_f32 %0, %1, %2 op_sel:[1,1] op_sel_hi:[0,0] neg_hi:[1,0]" : "=v"(a[4 + j]) : "v"(zp_[j]), "v"(zq_[j]));
    }
    bfly4<false>(a[0], a[1], a[2], a[3]);
    bfly4<false>(a[4], a[5], a[6], a[7]);
#pragma unroll
    for (int k = 1; k < 4; ++k) { a[k] = cmul(a[k], twa(k, 0)); a[4 + k] = cmul(a[4 + k], twa(k, 1)); }
#pragma unroll
    for (int k = 0; k < 4; ++k) *(LDS_AS f4*)(fb + k * 512 + 2 * t) = f4{a[k].x, a[k].y, a[4 + k].x, a[4 + k].y};
    twb[1] = twb1; twb[2] = cmul(twb1, twb1); twb[3] = cmul(twb[2], twb1); twb[4] = cmul(twb[2], twb[2]);
    twb[5] = cmul(twb[4], twb1); twb[6] = cmul(twb[3], twb[3]); twb[7] = cmul(twb[4], twb[3]);
    sync4(0);
#pragma unroll
    for (int i = 0; i < 8; ++i) a[i] = wr_[lane + 64 * i];
    twc[1] = twc1; twc[2] = cmul(twc1, twc1); twc[3] = cmul(twc[2], twc1); twc[4] = cmul(twc[2], twc[2]);
    twc[5] = cmul(twc[4], twc1); twc[6] = cmul(twc[3], twc[3]); twc[7] = cmul(twc[4], twc[3]);
    bfly8<false>(a);
    wr_[lane] = a[0];
#pragma unroll
    for (int k = 1; k < 8; ++k) wr_[k * 64 + (lane ^ ((k & 3) << 3))] = cmul(a[k], twb[k]);
    WAVE_SYNC();
#pragma unroll
    for (int i = 0; i < 8; ++i) a[i] = wr_[ka * 64 + u + 8 * (i ^ (ka & 3))];
    BULK(0, 5);
    bfly8<false>(a);
    const int bw = ka * 64 + ((ka & 3) << 3) + u;
    const int br = ka * 64 + ((u >> 2) << 5) + (((u ^ ka) & 3) << 3) + u;
    wr_[bw] = a[0];
#pragma unroll
    for (int k = 1; k < 8; ++k) wr_[bw ^ (((k >> 2) << 5) | ((k & 3) << 3) | k)] = cmul(a[k], twc[k]);
    WAVE_SYNC();
#pragma unroll
    for (int i = 0; i < 8; ++i) a[i] = wr_[br ^ i];
    BULK(5, 10);
    bfly8<false>(a);
#pragma unroll
    for (int c = 0; c < 8; ++c) {
      const u32 wv = c < 4 ? wv0[c & 3] : wv1[c & 3];
      a[c] = cmul(a[c], f2{bf_lo(wv), bf_hi(wv)});
    }
    bfly8<true>(a);
#pragma unroll
    for (int i = 0; i < 8; ++i) wr_[br ^ i] = a[i];
    WAVE_SYNC();
#pragma unroll
    for (int k = 0; k < 8; ++k) a[k] = wr_[bw ^ (((k >> 2) << 5) | ((k & 3) << 3) | k)];
    BULK(10, 15);
#pragma unroll
    for (int k = 1; k < 8; ++k) a[k] = cmulc(a[k], twc[k]);
    bfly8<true>(a);
#pragma unroll
    for (int i = 0; i < 8; ++i) wr_[ka * 64 + u + 8 * (i ^ (ka & 3))] = a[i];
    WAVE_SYNC();
#pragma unroll
    for (int k = 0; k < 8; ++k) a[k] = wr_[k * 64 + (lane ^ ((k & 3) << 3))];
    BULK(15, 19);
#pragma unroll
    for (int k = 1; k < 8; ++k) a[k] = cmulc(a[k], twb[k]);
    bfly8<true>(a);
#pragma unroll
    for (int i = 0; i < 8; ++i) wr_[lane + 64 * i] = a[i];
    BULK(19, 23);
    sync4(1);
#pragma unroll
    for (int k = 0; k < 4; ++k) { f4 v = *(LDS_AS f4*)(fb + k * 512 + 2 * t); a[k] = v.xy; a[4 + k] = v.zw; }
#pragma unroll
    for (int k = 1; k < 4; ++k) { a[k] = cmulc(a[k], twa(k, 0)); a[4 + k] = cmulc(a[4 + k], twa(k, 1)); }
    bfly4<true>(a[0], a[1], a[2], a[3]);
    bfly4<true>(a[4], a[5], a[6], a[7]);
    const auto zrs = __builtin_amdgcn_make_buffer_rsrc((void*)Z2T, 0, 576 * 1024 * 8, 0x00020000);
    const bool self = task == 0 || task == 288;
#pragma unroll
    for (int j = 0; j < 4; ++j) {
      f2 r0 = add_pi(a[j], a[4 + j]), r1;
      asm("v_pk_add_f32 %0, %1, %2 op_sel:[0,1] op_sel_hi:[1,0] neg_hi:[1,0]" : "=v"(r1) : "v"(a[j]), "v"(a[4 + j]));
      if (self) { r0 = f2{a[j].x, a[4 + j].x}; r1 = r0; }
      __builtin_amdgcn_raw_buffer_store_b64(__builtin_bit_cast(u2v, r0), zrs, t * 8 + j * 2048, task * 8192, 16);
      __builtin_amdgcn_raw_buffer_store_b64(__builtin_bit_cast(u2v, r1), zrs, t * 8 + j * 2048, (task == 0 ? 0 : 576 - task) * 8192, 16);
    }
    asm volatile("" ::: "memory");
  } else {
    STAMP(2);
    if (grp >= G_ONE) BULK(0, 5);
    BULK(5, 23);
  }
  STAMP(3);
  asm volatile("s_waitcnt vmcnt(18)" : "+v"(bias_raw) :: "memory");
  if (tid >= 224) {
    const int bt = tid - 224;
    if (bt < 256) *(LDS_AS float*)(lds + L_B1 + bt * 4) = bf_lo(bias_raw);
    else {
      const float hb = 0.5f * bf_lo(bias_raw);
      *(LDS_AS float*)(lds + L_B1 + bt * 4) = hb;
      *(LDS_AS float*)(lds + L_B1 + 128 + (bt ^ 16) * 4) = hb;
    }
  }
  delay_until(my_dly);
  RAW_BAR();
  STAMP(4);

  const int blkx = blk ^ hf;
  const int colB = ((4 * hf + 2 * blk + (p4 >> 1)) ^ ((q >> 1) << 2)) * 16 + (p4 & 1) * 8;
  const int baseA0 = (8 * h + q) * 128 + colB + hf * 2048;
  const int baseA1 = (8 * h + q) * 128 + colB - hf * 2048;
  const int baseS = hf * 1024 + (8 * h + q) * 64 + blkx * 32 + p4 * 8;
  const int baseB = (4 * h + q) * 128 + colB;
  int hx_w = L_H + hf * 8192 + mi * 2048 + lane * 16;
  int hx_r = L_H + mi * 2048 + lane * 16;
  int xs_own = L_H + 16384 + w * 2048 + lane * 16;
  int xs_oth = L_H + 16384 + (w ^ 4) * 2048 + lane * 16;
  int b1_base = L_B1 + hf * 128 + 16 * h;
  PIN(hx_w); PIN(hx_r); PIN(xs_own); PIN(xs_oth); PIN(b1_base);
  const auto prs = __builtin_amdgcn_make_buffer_rsrc((void*)P, 0, 16 * S_ * D_ * 2, 0x00020000);
  const u32 pvoff = ((u32)((lane & 31) >> 3) * 16 * 576 + h * 8 + (lane & 7)) * 16;
  const u32 psoff = (u32)(((16 * mt + 4 * mi) * 16 + fs) * 576 + hf * 32) * 16;

  s8v wf[324];
  f16v acc1[5];
  f16v acc2[9];
  float gx[5][16], gt[5][16];
  u4v hpk[5][2];
  u4v hq[9][2];
  f4 xin[2];

  int cb0, cb1;
  auto rd = [&](auto NN) {
    constexpr int n = decltype(NN)::value, st = n / 18, pos = n % 18;
    constexpr int slot = (st % 3) * SLOT;
    if constexpr (pos == 0) {
      cb0 = (st == 0 ? baseS : (st <= 8 ? baseA0 : baseB)) + slot;
      cb1 = (st <= 8 ? baseA1 : baseB + hf * 2048) + slot;
      PIN(cb0); PIN(cb1);
    }
    int a0, a1;
    if constexpr (st == 0) { a0 = cb0 + pos * 2048; a1 = a0 + 256; }
    else if constexpr (st <= 8) { a0 = ((pos & 1) ? cb1 : cb0) + pos * 2048; a1 = a0 + 512; }
    else {
      constexpr int tl = pos / 6, rem = pos % 6;
      constexpr int u = st <= 11 ? 3 * (rem % 3) + tl : pos >> 1;
      constexpr int s = st <= 11 ? rem / 3 : pos & 1;
      constexpr int tau = st <= 11 ? 3 * (st - 9) + tl : pos >> 1;
      if constexpr (tau == 0) a0 = (s ? (cb1 ^ 2048) : cb1) + u * 4096;
      else a0 = cb0 + u * 4096 + s * 2048;
      a1 = a0 + 1024;
    }
    s4v lo = __builtin_amdgcn_ds_read_tr16_b64_v4i16((LDS_AS s4v*)(lds + a0));
    s4v hi = __builtin_amdgcn_ds_read_tr16_b64_v4i16((LDS_AS s4v*)(lds + a1));
    wf[n] = __builtin_shufflevector(lo, hi, 0, 1, 2, 3, 4, 5, 6, 7);
  };
  const f16v zero16 = {0, 0, 0, 0, 0, 0, 0, 0, 0, 0, 0, 0, 0, 0, 0, 0};
#define MFMA(A, B, C) __builtin_amdgcn_mfma_f32_32x32x16_bf16(__builtin_bit_cast(bf8v, A), __builtin_bit_cast(bf8v, B), C, 0, 0, 0)
  auto mm = [&](auto NN) {
    constexpr int n = decltype(NN)::value, st = n / 18, pos = n % 18;
    if constexpr (st == 0) acc1[0] = MFMA(wf[n], yf[2 * pos], acc1[0]);
    else if constexpr (st <= 8) {
      constexpr int p = (st - 1) >> 1, half = (st - 1) & 1;
      acc1[1 + p] = MFMA(wf[n], yf[18 * half + pos], acc1[1 + p]);
    } else if constexpr (st <= 11) {
      constexpr int tl = pos / 6, rem = pos % 6, bb = rem % 3, s = rem / 3, tau = 3 * (st - 9) + tl;
      acc2[bb] = MFMA(wf[n], hq[tau][s], (st == 9 && pos < 3) ? zero16 : acc2[bb]);
    } else {
      constexpr int j = st - 9;
      acc2[j] = MFMA(wf[n], hq[pos >> 1][pos & 1], pos == 0 ? zero16 : acc2[j]);
    }
  };
  auto acc_init = [&](auto JOB) {
    constexpr int job = decltype(JOB)::value, off = job == 0 ? 1024 : (job - 1) * 256;
    const f4 v0 = *(LDS_AS f4*)(lds + b1_base + off), v1 = *(LDS_AS f4*)(lds + b1_base + off + 32);
    const f4 v2 = *(LDS_AS f4*)(lds + b1_base + off + 64), v3 = *(LDS_AS f4*)(lds + b1_base + off + 96);
    acc1[job] = f16v{v0[0], v0[1], v0[2], v0[3], v1[0], v1[1], v1[2], v1[3], v2[0], v2[1], v2[2], v2[3], v3[0], v3[1], v3[2], v3[3]};
  };
  auto gelu_op = [&](auto JOB, auto EE, auto JJ) {
    constexpr int job = decltype(JOB)::value, e = decltype(EE)::value, j = decltype(JJ)::value;
    float& x = gx[job][e]; float& t_ = gt[job][e];
    if constexpr (j == 0) { if constexpr (job == 0) { x = acc1[0][e] + xin[e >> 2][e & 3]; PIN(x); } else x = acc1[job][e]; }
    else if constexpr (j == 1) { t_ = x * x; PIN(t_); }
    else if constexpr (j == 2) { t_ = __builtin_fmaf(t_, -0.1029432397f, -2.3022081983651f); PIN(t_); }
    else if constexpr (j == 3) { t_ = t_ * x; PIN(t_); }
    else if constexpr (j == 4) { t_ = __builtin_amdgcn_exp2f(t_); PIN(t_); }
    else if constexpr (j == 5) { t_ = t_ + 1.0f; PIN(t_); }
    else if constexpr (j == 6) { t_ = __builtin_amdgcn_rcpf(t_); PIN(t_); }
    else if constexpr (j == 7) { x = x * t_; PIN(x); }
    else if constexpr (e & 1) { u32 pk = pack_bf2(gx[job][e - 1], x); PIN(pk); hpk[job][e >> 3][(e >> 1) & 3] = pk; }
  };
  auto h_read = [&](auto TAU, auto SS) {
    constexpr int tau = decltype(TAU)::value, s = decltype(SS)::value;
    hq[tau][s] = *(LDS_AS u4v*)(lds + hx_r + ((tau - 1) % 6) * 8192 + s * 1024);
  };
#define SB() __builtin_amdgcn_sched_barrier(0)
  u4v pk[18];
  const int rb = 16 * mt + fs;
  gu32* cntP = (gu32*)sync + mt * SYNC_STRIDE;
  gu32* cntZ = (gu32*)sync + 32 * SYNC_STRIDE;
  gu32* cntQ = (gu32*)sync + (16 + mt) * SYNC_STRIDE;
  f2 za[8]; f4 tw64p[4], twp[4]; u32 xr[9], zpoll, tvz, tvx;
  auto tload = [&](auto KK) {
    constexpr int k = decltype(KK)::value;
    if constexpr (k < 0) {
      const u32* cz = sync + 32 * SYNC_STRIDE; u32& dst = zpoll; const u32 zero = 0;
      asm volatile("global_load_dword %0, %1, %2 sc1" : "=v"(dst) : "v"(zero), "s"(cz) : "memory");
    } else if constexpr (k < 8) {
      if constexpr (k == 0) {
        u32& zp = zpoll;
        PIN(zp);
        if ((u32)__builtin_amdgcn_readfirstlane(zpoll) < (u32)N_FFT_WG) wait_count(cntZ, N_FFT_WG);
        int t2 = tid; PIN(t2);
        const int ft = t2 < 288 ? t2 : 287;
        tvz = (((ft >> 5) + 9 * ((ft >> 2) & 7)) * 1024 + rb * 4 + (ft & 3)) * 8;
      }
      f2& dst = za[k]; const u32 vo = tvz; const u4v srd = mk_srd(Z2T, 576 * 1024 * 8);
      asm volatile("buffer_load_dwordx2 %0, %1, %2, %3 offen sc1" : "=v"(dst) : "v"(vo), "s"(srd), "s"(k * 72 * 8192) : "memory");
    } else if constexpr (k < 12) { int t2 = tid; PIN(t2); gl16(tw64p[k - 8], &g_twr.b[k - 8][0][0], ((t2 >> 2) & 7) * 16); }
    else if constexpr (k < 16) { int l2 = lane; PIN(l2); gl16(twp[k - 12], &g_twr.a[k - 12][0][0], l2 * 16); }
    else {
      if constexpr (k == 16) { int t2 = tid; PIN(t2); tvx = ((rb * 8 + mi * 2 + ((t2 >> 5) & 1)) * D_ + 2 * (t2 & 31)) * 2; }
      u32& dst = xr[k - 16]; const u32 vo = tvx; const u16* xb = x;
      asm volatile("global_load_dword %0, %1, %2 offset:%3" : "=v"(dst) : "v"(vo), "s"(xb), "n"((k - 16) * 128) : "memory");
    }
  };
  auto step = [&](auto NN) {
    constexpr int n = decltype(NN)::value, iv = n / 18, pos = n % 18;
    constexpr VmOp vo = vm_at(n);
    if constexpr (pos == 0 && iv >= 1 && iv < 18) {
      asm volatile("s_waitcnt vmcnt(%0)" ::"n"(vm_wait(iv)) : "memory");
      RAW_BAR();
      STAMP(4 + iv);
      if constexpr (iv == 2 || iv == 17) {
        const u32* cw = sync + (iv == 2 ? 32 : 16 + mt) * SYNC_STRIDE; const u32 zero = 0, one = 1; const int w_ = w;
        asm volatile("s_cmp_lg_u32 %3, 0\n\ts_cbranch_scc1 1f\n\ts_mov_b64 exec, 1\n\tglobal_atomic_add %0, %1, %2\n\ts_mov_b64 exec, -1\n1:"
                     :: "v"(zero), "v"(one), "s"(cw), "s"(w_) : "memory", "scc");
      }
    }
    if constexpr (n == 0) acc_init(IC<0>{});
    if constexpr (iv < 18 && pos < 15) {
      constexpr int r0 = (6 * pos + 4) / 5;
      rd(IC<iv * 18 + r0>{});
      if constexpr ((5 * (r0 + 1)) / 6 == pos) rd(IC<iv * 18 + r0 + 1>{});
    }
    if constexpr (iv <= 6 && (iv & 1) == 0 && pos == 15) acc_init(IC<(iv <= 6 ? 1 + iv / 2 : 0)>{});
    if constexpr (n == NS(2, 1)) { xin[0] = *(LDS_AS f4*)(lds + xs_own); xin[1] = *(LDS_AS f4*)(lds + xs_own + 1024); }
    if constexpr (n == NS(4, 2)) hq[0][1] = *(LDS_AS u4v*)(lds + xs_oth + 16384);
    if constexpr (iv == 8 && pos >= 6 && pos < 14) h_read(IC<(iv == 8 && pos >= 6 && pos < 14) ? 1 + (pos - 6) / 2 : 1>{}, IC<pos & 1>{});
    if constexpr (iv == 10 && pos >= 2 && pos < 6) h_read(IC<(iv == 10 && pos >= 2 && pos < 6) ? 5 + (pos - 2) / 2 : 1>{}, IC<pos & 1>{});
    if constexpr (iv == 11 && pos >= 1 && pos < 5) h_read(IC<(iv == 11 && pos >= 1 && pos < 5) ? 7 + (pos - 1) / 2 : 1>{}, IC<(pos - 1) & 1>{});
    if constexpr (n == NS(1, 15)) *(LDS_AS f4*)(lds + xs_oth) = f4{acc1[0][8], acc1[0][9], acc1[0][10], acc1[0][11]};
    if constexpr (n == NS(1, 16)) *(LDS_AS f4*)(lds + xs_oth + 1024) = f4{acc1[0][12], acc1[0][13], acc1[0][14], acc1[0][15]};
    if constexpr (n == h_wr(0, 0)) { *(LDS_AS u4v*)(lds + xs_own + 16384) = hpk[0][0]; hq[0][0] = hpk[0][0]; }
    static_for<1, 5>([&](auto JOB) {
      constexpr int job = decltype(JOB)::value;
      if constexpr (n == h_wr(job, 0)) *(LDS_AS u4v*)(lds + hx_w + ((job - 1) % 3) * 16384) = hpk[job][0];
      if constexpr (n == h_wr(job, 1)) *(LDS_AS u4v*)(lds + hx_w + ((job - 1) % 3) * 16384 + 1024) = hpk[job][1];
    });
    SB();
    static_for<0, 2>([&](auto PH) {
      static_for<0, 5>([&](auto JOB) {
        constexpr int job = decltype(JOB)::value;
        if constexpr (n >= g_start(job) && n < g_write(job, job == 0 ? 0 : 1)) {
          static_for<0, job == 0 ? 8 : 16>([&](auto EE) {
            constexpr int j = n - g_elem(job, decltype(EE)::value);
            if constexpr (j >= 0 && j <= 8 && ((j == 4 || j == 6) == (decltype(PH)::value == 0))) gelu_op(JOB, EE, IC<j>{});
          });
        }
      });
      SB();
    });
    if constexpr (vm_at(n + 1).kind == VM_ST) {
      constexpr int k = vm_at(n + 1).a, j = k >> 1, s2 = k & 1;
      const f16v& a = acc2[j];
      pk[k] = u4v{pack_bf2(a[8 * s2], a[8 * s2 + 1]), pack_bf2(a[8 * s2 + 2], a[8 * s2 + 3]),
                  pack_bf2(a[8 * s2 + 4], a[8 * s2 + 5]), pack_bf2(a[8 * s2 + 6], a[8 * s2 + 7])};
      PIN(pk[k]);
      SB();
    }
    if constexpr (vo.kind == VM_DMA) dma(IC<vo.a>{}, IC<vo.b>{});
    if constexpr (vo.kind == VM_Y) yload(IC<vo.a>{});
    if constexpr (vo.kind == VM_TL) tload(IC<vo.a>{});
    if constexpr (vo.kind == VM_PF) {
      int l2 = lane; PIN(l2);
      const u32 ln = (u32)((grp * 8 + w) * 24 + vo.a * 8) + (u32)(l2 & 7), lc = ln < 1440 ? ln : 1439, r = lc / 5;
      const u32 off = (r < 256 ? (u32)fpan + r : (u32)fsgl + r - 256) * (D_ * 2) + 512 + (lc - r * 5) * 128;
      l2_touch(lds0 + L_DUMMY, off, srd2);
    }
    if constexpr (vo.kind == VM_ST) {
      u32 so = psoff; SPIN(so);
      __builtin_amdgcn_raw_buffer_store_b128(pk[vo.a], prs, pvoff, so + ((vo.a >> 1) * 64 + (vo.a & 1) * 16) * 16, P_AUX);
    }
    SB();
    if constexpr (n >= PF && n - PF < 324) {
      constexpr int m = n - PF, yi = m < 18 ? 2 * m : m - 18;
      if constexpr (m < 54 && (m < 18 || (yi & 1))) {
        u4v& yr = yf[yi];
        asm volatile("s_waitcnt vmcnt(%1)" : "+v"(yr) : "n"(vm_wait_y(yi, n)) : "memory");
      }
      mm(IC<m>{});
    }
    SB();
  };
  static_for<0, 166>(step);
  static_for<166, 324 + 8>(step);
  STAMP(22);

  const int l31 = lane & 31;
  LDS_AS f2* buf = (LDS_AS f2*)(lds + L_H);
  LDS_AS u32* SX = (LDS_AS u32*)(lds + L_H + 18432);
  LDS_AS u32* c_drain = (LDS_AS u32*)(lds + L_CNT + 16);
  LDS_AS u32* c_fft = c_drain + 1; LDS_AS u32* c_sx = c_drain + 2; LDS_AS u32* c_go = c_drain + 3;
  auto lds_inc = [&](LDS_AS u32* c) __attribute__((always_inline)) { if (lane == 0) __hip_atomic_fetch_add(c, 1u, __ATOMIC_RELAXED, __HIP_MEMORY_SCOPE_WORKGROUP); };
  auto lds_wait = [&](LDS_AS u32* c, u32 n) __attribute__((always_inline)) {
    while (__builtin_amdgcn_readfirstlane(__hip_atomic_load(c, __ATOMIC_RELAXED, __HIP_MEMORY_SCOPE_WORKGROUP)) < n) __builtin_amdgcn_s_sleep(1);
    asm volatile("" ::: "memory");
  };
  const int pi = lane & 7, ph = (lane >> 3) & 1;
  const u4v psrd = mk_srd(P + (size_t)rb * 16 * 576, 16 * 9216);
  auto reduce = [&](auto NU, int u0) __attribute__((always_inline)) {
    constexpr int nu = decltype(NU)::value;
    u4v pv[nu][16];
    u2v bq[nu][2];
    const u32 vo = (u0 * 64 + lane) * 16, vb = ((u0 * 4 + (lane >> 4)) * 16 + 4 * ph) * 2;
    static_for<0, 2 * nu>([&](auto KK) {
      constexpr int k = decltype(KK)::value;
      u2v& dst = bq[k >> 1][k & 1]; const u32 vb_ = vb; const u16* b2_ = b2;
      asm volatile("global_load_dwordx2 %0, %1, %2 offset:%3" : "=v"(dst) : "v"(vb_), "s"(b2_), "n"((k >> 1) * 128 + (k & 1) * 16) : "memory");
    });
    static_for<0, 16 * nu>([&](auto FF) {
      constexpr int k = decltype(FF)::value / 16, f = decltype(FF)::value % 16;
      u4v& dst = pv[k][f]; const u32 vo_ = vo; const u4v srd_ = psrd;
      asm volatile("buffer_load_dwordx4 %0, %1, %2, %3 offen offset:%4 sc1" : "=v"(dst) : "v"(vo_), "s"(srd_), "s"(f * 9216), "n"(k * 1024) : "memory");
    });
    static_for<0, nu>([&](auto KK) {
      constexpr int k = decltype(KK)::value;
      f2 sm2[4];
#pragma unroll
      for (int e = 0; e < 4; ++e) sm2[e] = f2{0.f, 0.f};
      static_for<0, 16>([&](auto FF) {
        constexpr int f = decltype(FF)::value;
        u4v& pr = pv[k][f];
        asm volatile("s_waitcnt vmcnt(%1)" : "+v"(pr) : "n"(16 * nu - 1 - 16 * k - f + k) : "memory");
#pragma unroll
        for (int e = 0; e < 4; ++e) { const u32 pe = pr[e]; sm2[e] += f2{bf_lo(pe), bf_hi(pe)}; PIN(sm2[e]); }
      });
      PIN(bq[k][0]); PIN(bq[k][1]);
      if constexpr (k == 0) lds_wait(c_sx, 4u);
      const int c16 = (u0 + k) * 4 + (lane >> 4), dd0 = c16 * 16 + 4 * ph;
      const u4v sx0 = *(LDS_AS u4v*)(SX + pi * 580 + dd0), sx1 = *(LDS_AS u4v*)(SX + pi * 580 + dd0 + 8);
      float o[8];
#pragma unroll
      for (int e = 0; e < 8; ++e) {
        const u32 bw = bq[k][e >> 2][(e & 3) >> 1];
        const float bv = (e & 1) ? bf_hi(bw) : bf_lo(bw);
        const float m = rbf(rbf(sm2[e >> 1][e & 1]) + bv);
        const float g = rbf(m * __builtin_amdgcn_rcpf(1.0f + __builtin_amdgcn_exp2f(-1.4426950408889634f * m)));
        const u32 sx = e < 4 ? sx0[e & 3] : sx1[e & 3];
        o[e] = bf_hi(sx) + rbf(bf_lo(sx) * g);
      }
      const u32 a0 = pack_bf2(o[0], o[1]), a1 = pack_bf2(o[2], o[3]), b0 = pack_bf2(o[4], o[5]), b1_ = pack_bf2(o[6], o[7]);
      const u32 r0 = __builtin_amdgcn_update_dpp(0, (int)(ph ? a0 : b0), 0x128, 0xf, 0xf, true);
      const u32 r1 = __builtin_amdgcn_update_dpp(0, (int)(ph ? a1 : b1_), 0x128, 0xf, 0xf, true);
      const u4v ov = ph ? u4v{r0, r1, b0, b1_} : u4v{a0, a1, r0, r1};
      *(GLB_AS u4v*)(out + (size_t)(rb * 8 + pi) * D_ + c16 * 16 + 8 * ph) = ov;
      asm volatile("" ::: "memory");
    });
  };
  asm volatile("s_waitcnt vmcnt(2)" ::: "memory");
#pragma unroll
  for (int i = 0; i < 8; ++i) PIN(za[i]);
#pragma unroll
  for (int i = 0; i < 4; ++i) { PIN(tw64p[i]); PIN(twp[i]); }
#pragma unroll
  for (int i = 0; i < 9; ++i) PIN(xr[i]);
  if (w < 5) {
    if (tid < 288) {
      const int k9 = tid >> 5, tt = (tid >> 2) & 7, fw = tid & 3;
      LDS_AS f2* rg = buf + k9 * 256;
      bfly8<true>(za);
#pragma unroll
      for (int i = 0; i < 8; ++i) rg[tt * 32 + ((i ^ tt) * 4 + fw)] = za[i];
      WAVE_SYNC();
      za[0] = rg[tt * 4 + fw];
#pragma unroll
      for (int k = 1; k < 8; ++k) za[k] = cmulc(rg[k * 32 + ((tt ^ k) * 4 + fw)], ((k - 1) & 1) ? tw64p[(k - 1) >> 1].zw : tw64p[(k - 1) >> 1].xy);
      bfly8<true>(za);
      const int e = tt * 4 + fw, e2 = e ^ ((e >> 4) << 2);
#pragma unroll
      for (int i = 0; i < 8; ++i) rg[i * 32 + (e2 ^ (i & 3))] = za[i];
    }
    asm volatile("s_waitcnt vmcnt(0)" ::: "memory");
    lds_inc(c_drain);
    STAMP(23);
    asm volatile("s_waitcnt lgkmcnt(0)" ::: "memory");
    lds_inc(c_fft);
    if (w == 4) {
      __builtin_amdgcn_s_setprio(3);
      lds_wait(c_drain, 8u);
      if (lane == 0) __hip_atomic_fetch_add(cntP, 1u, RLX_AGENT);
      wait_count(cntP, 16u);
      lds_inc(c_go);
    } else {
      lds_wait(c_fft, 5u);
      f2 c[9];
      const int n0 = 2 * l31 + h;
      const int i = n0 >> 3, e = (n0 & 7) * 4 + w;
      const int pos = i * 32 + (e ^ ((e >> 4) << 2) ^ (i & 3));
      c[0] = buf[pos];
#pragma unroll
      for (int k = 1; k < 9; ++k) c[k] = cmulc(buf[k * 256 + pos], ((k - 1) & 1) ? twp[(k - 1) >> 1].zw : twp[(k - 1) >> 1].xy);
      bfly9<true>(c);
      LDS_AS u32* sx = SX + (w * 2 + h) * 580 + 2 * l31;
#pragma unroll
      for (int j = 0; j < 9; ++j) {
        const f2 cs = c[j] * (0.5f / ((float)S_ * (float)D_));
        auto r = __builtin_amdgcn_permlane32_swap(__float_as_uint(cs.x), __float_as_uint(cs.y), false, false);
        const u32 sp = pack_bf2(__uint_as_float(r[0]), __uint_as_float(r[1]));
        *(LDS_AS u2v*)(sx + 64 * j) = u2v{(sp & 0xffffu) | (xr[j] << 16), (sp >> 16) | (xr[j] & 0xffff0000u)};
      }
      asm volatile("s_waitcnt lgkmcnt(0)" ::: "memory");
      lds_inc(c_sx);
    }
    STAMP(24);
    if (w < 3) { lds_wait(c_go, 1u); STAMP(25); reduce(IC<1>{}, 6 + w); } else { STAMP(25); }
  } else {
    const u32 q0 = __hip_atomic_load(cntQ, RLX_AGENT);
    __builtin_amdgcn_s_waitcnt(0x0F70);
    asm volatile("s_waitcnt vmcnt(0)" ::: "memory");
    lds_inc(c_drain);
    STAMP(23);
    if ((u32)__builtin_amdgcn_readfirstlane(q0) < 16u) wait_count(cntQ, 16u);
    __builtin_amdgcn_fence(__ATOMIC_ACQUIRE, "wavefront");
    __builtin_amdgcn_s_waitcnt(0x0F70);
    STAMP(24); STAMP(25);
    reduce(IC<2>{}, (w - 5) * 2);
  }
  STAMP(26);
  asm volatile(".Lk1_end:");
}

extern "C" void kernel_launch(void* const* d_in, const int* in_sizes, int n_in, void* d_out, int out_size, void* d_ws,
                              size_t ws_size, hipStream_t stream) {
  const u16* x = (const u16*)d_in[0];
  const u16* wr = (const u16*)d_in[1];
  const u16* wi = (const u16*)d_in[2];
  const u16* w1 = (const u16*)d_in[4];
  const u16* b1 = (const u16*)d_in[5];
  const u16* w2 = (const u16*)d_in[6];
  const u16* b2 = (const u16*)d_in[7];
  (void)hipFuncSetAttribute((const void*)k1_main, hipFuncAttributeMaxDynamicSharedMemorySize, L_TOTAL);
  k0_prep<<<512, 320, 0, stream>>>(x, wr, wi, w1, (char*)d_ws);
  k1_main<<<256, 512, L_TOTAL, stream>>>(w1, b1, w2, b2, x, (char*)d_ws, (u16*)d_out);
}
```
